```python
import torch

def _softmax(x, axis=-1):
    return torch.softmax(x, dim=axis)
def _gelu(x):
    return torch.nn.functional.gelu(x, approximate="tanh")
def _silu(x):
    return torch.nn.functional.silu(x)
def _sigmoid(x):
    return torch.sigmoid(x)
def _layernorm(x, axis=-1, eps=1e-5):
    m = x.mean(dim=axis, keepdim=True)
    v = x.var(dim=axis, keepdim=True, unbiased=False)
    return (x - m) / torch.sqrt(v + eps)
def _scatter_add(tgt, idx, src, axis=0):
    return tgt.clone().index_add_(axis, idx, src)
def _segment_sum(src, seg, n):
    out = torch.zeros((n,) + src.shape[1:], dtype=src.dtype)
    return out.index_add_(0, seg, src)
def _pad(x, pw):
    flat = tuple(v for pair in reversed(pw) for v in pair)
    return torch.nn.functional.pad(x, flat)

kernel_name = 'concat_heads__ssm_conv_s4d__spectral_gfnet__prompt'

def setup_inputs():

    _w = (lambda a: (lambda t: t.to(torch.bfloat16) if t.is_floating_point() else t)(torch.as_tensor(a)))
    def _A_setup():
        Bsz, L, D, N = 1, 2048, 1024, 128
        return {
            "u": _w((lambda *s: torch.randn(*s, dtype=torch.bfloat16) * 0.05)(Bsz, L, D)),
            "log_A_real": _w((lambda *s: torch.randn(*s, dtype=torch.bfloat16) * 6.4)(D, N) * 0.5),
            "A_imag": _w((lambda *s: torch.randn(*s, dtype=torch.bfloat16) * 0.05)(D, N) * 3.14),
            "C_real": _w((lambda *s: torch.randn(*s, dtype=torch.bfloat16) * 0.05)(D, N) / N ** 0.5),
            "C_imag": _w((lambda *s: torch.randn(*s, dtype=torch.bfloat16) * 0.05)(D, N) / N ** 0.5),
            "log_dt": _w((lambda *s: torch.randn(*s, dtype=torch.bfloat16) * 0.05)(D) * 0.5 - 4.0),
            "D_skip": _w((lambda *s: torch.randn(*s, dtype=torch.bfloat16) * 0.05)(D)),
        }
    _A_in = _A_setup()

    def _B_setup():
        B, S, D = 1, 2048, 1024
        Df = D // 2 + 1
        return {
            "x": _w((lambda *s: torch.randn(*s, dtype=torch.bfloat16) * 0.05)(B, S, D)),
            "w_r": _w((lambda *s: torch.randn(*s, dtype=torch.bfloat16) * 0.8)(S, Df) * 0.02),
            "w_i": _w((lambda *s: torch.randn(*s, dtype=torch.bfloat16) * 0.8)(S, Df) * 0.02),
        }
    _B_in = _B_setup()

    return {
        "A_u": _A_in["u"],
        "A_log_A_real": _A_in["log_A_real"],
        "A_A_imag": _A_in["A_imag"],
        "A_C_real": _A_in["C_real"],
        "A_C_imag": _A_in["C_imag"],
        "A_log_dt": _A_in["log_dt"],
        "A_D_skip": _A_in["D_skip"],
        "B_x": _B_in["x"],
        "B_w_r": _B_in["w_r"],
        "B_w_i": _B_in["w_i"],
    }

def reference(A_u, A_log_A_real, A_A_imag, A_C_real, A_C_imag, A_log_dt, A_D_skip, B_x, B_w_r, B_w_i):
    def _A_ref(u, log_A_real, A_imag, C_real, C_imag, log_dt, D_skip):
        L = u.shape[1]
        dt = torch.exp(log_dt)
        A = -torch.exp(log_A_real) + 1j * A_imag
        C = (C_real + 1j * C_imag) * dt[:, None]
        pos = (lambda x, dt: x.to(dt))(torch.arange(L), log_dt.dtype)
        P = torch.exp((A * dt[:, None])[:, :, None] * pos[None, None, :])
        K = 2.0 * torch.einsum("dn,dnl->dl", C, P).real
        L2 = 2 * L
        u_t = (lambda x, *d: x.permute(*d))(u, 0, 2, 1)
        Kf = (lambda x, *a, **k: torch.fft.rfft(x.float(), *a, **k))(K, L2)
        Uf = (lambda x, *a, **k: torch.fft.rfft(x.float(), *a, **k))(u_t, L2)
        y = (lambda x, *a, **k: torch.fft.irfft(x, *a, **k).to(torch.bfloat16))(Uf * Kf[None, :, :], L2)[:, :, :L]
        y = (lambda x, *d: x.permute(*d))(y, 0, 2, 1)
        return y + u * D_skip[None, None, :]

    def _B_ref(x, w_r, w_i):
        B, S, D = x.shape
        X = (lambda x, axes=(-2,-1): torch.fft.rfft2(x.float(), dim=axes))(x, axes=(-2, -1))
        W = w_r + 1j * w_i
        X = X * W
        y = (lambda x, s, axes=(-2,-1): torch.fft.irfft2(x, s=s, dim=axes).to(torch.bfloat16))(X, (S, D), axes=(-2, -1))
        return y

    a = _A_ref(A_u, A_log_A_real, A_A_imag, A_C_real, A_C_imag, A_log_dt, A_D_skip)
    b = _B_ref(B_x, B_w_r, B_w_i)
    return torch.concatenate([a, b], axis=-1)
```

```cpp
#include <hip/hip_runtime.h>
#include <stdint.h>
#include <type_traits>

#define DI __device__ __forceinline__
typedef __bf16 bf16x8 __attribute__((ext_vector_type(8)));
typedef __bf16 bf16x2 __attribute__((ext_vector_type(2)));
typedef float f32x4 __attribute__((ext_vector_type(4)));
typedef float f32x2 __attribute__((ext_vector_type(2)));
typedef unsigned u32x4 __attribute__((ext_vector_type(4)));
typedef unsigned u32x2 __attribute__((ext_vector_type(2)));
typedef __amdgpu_buffer_rsrc_t rsrc_t;
typedef unsigned long long u64;

constexpr int SC1 = 16, NT = 2;
constexpr unsigned TAG_INC = 0x9E3779B9u;
constexpr int SPIN_LIMIT = 1 << 20;
constexpr int WS_Z = 0;
constexpr int WS_UT = 4 << 20;
constexpr int WS_FL = 8 << 20;
constexpr int FL_EB = 0, FL_EA = 4096, FL_A1 = 16384, FL_B1 = 32768, FL_BYTES = 65536, FS = 16;
constexpr int A_SLICE = 20160;
constexpr int LDS_BYTES = 81152;
static_assert(4 * A_SLICE <= LDS_BYTES, "lds");

#define PIN(x) asm volatile("" : "+v"(x))
#define PIN4(x) do { float t0_ = x[0], t1_ = x[1], t2_ = x[2], t3_ = x[3]; asm volatile("" : "+v"(t0_), "+v"(t1_), "+v"(t2_), "+v"(t3_)); x[0] = t0_; x[1] = t1_; x[2] = t2_; x[3] = t3_; } while (0)
#define PINS(x) asm volatile("" : "+s"(x))
#define SB() __builtin_amdgcn_sched_barrier(0)
#define MIX(n) do { _Pragma("unroll") for (int i_ = 0; i_ < (n); ++i_) { __builtin_amdgcn_sched_group_barrier(0x8, 1, 0); __builtin_amdgcn_sched_group_barrier(0x2, 2, 0); } } while (0)
template <int I, int N, class F> DI void sfor(F&& f) { if constexpr (I < N) { f(std::integral_constant<int, I>{}); sfor<I + 1, N>(f); } }

DI unsigned pk(float lo, float hi) { f32x2 v = {lo, hi}; bf16x2 b = __builtin_convertvector(v, bf16x2); return __builtin_bit_cast(unsigned, b); }
DI f32x4 mma(u32x4 a, u32x4 b, f32x4 c) { return __builtin_amdgcn_mfma_f32_16x16x32_bf16(__builtin_bit_cast(bf16x8, a), __builtin_bit_cast(bf16x8, b), c, 0, 0, 0); }
DI f32x4 mma0(u32x4 a, u32x4 b) { f32x4 z = {0.f, 0.f, 0.f, 0.f}; return mma(a, b, z); }
DI rsrc_t mk(const void* p, int bytes) { return __builtin_amdgcn_make_buffer_rsrc((void*)p, 0, bytes, 0x00020000); }
DI float bflo(unsigned v) { return __uint_as_float(v << 16); }
DI float bfhi(unsigned v) { return __uint_as_float(v & 0xffff0000u); }
DI unsigned plo(unsigned a, unsigned b) { return __builtin_amdgcn_perm(b, a, 0x05040100u); }
DI unsigned phi(unsigned a, unsigned b) { return __builtin_amdgcn_perm(b, a, 0x07060302u); }
DI float cosr(float r) { return __builtin_amdgcn_cosf(r); }
DI float sinr(float r) { return __builtin_amdgcn_sinf(r); }
DI void drain() { asm volatile("s_waitcnt vmcnt(0)" ::: "memory"); }
DI void wave_sync() { __builtin_amdgcn_fence(__ATOMIC_ACQ_REL, "wavefront"); __builtin_amdgcn_wave_barrier(); }
DI void lds_barrier() { asm volatile("s_waitcnt lgkmcnt(0)\n\ts_barrier" ::: "memory"); }
DI u32x4 pk4(f32x4 re, f32x4 im) { u32x4 r = {pk(re[0], im[0]), pk(re[1], im[1]), pk(re[2], im[2]), pk(re[3], im[3])}; return r; }
DI unsigned sld(const void* p) { unsigned v = *(const __attribute__((address_space(4))) unsigned*)p; asm volatile("" : "+s"(v)); return v; }
DI float opq(float x) { asm("" : "+v"(x)); return x; }
DI f32x4 dot2(f32x4 a, f32x4 c, f32x4 b, f32x4 d) {
  f32x4 o;
#pragma unroll
  for (int i = 0; i < 4; ++i) o[i] = opq(__builtin_fmaf(a[i], c[i], opq(b[i] * d[i])));
  return o;
}

DI f32x4 dot2n(f32x4 a, f32x4 c, f32x4 b, f32x4 d) {
  f32x4 o;
#pragma unroll
  for (int i = 0; i < 4; ++i) o[i] = opq(__builtin_fmaf(a[i], c[i], -opq(b[i] * d[i])));
  return o;
}
template <int N, int DELAY> DI void wait_flags(rsrc_t rf, int off, unsigned tag) {
  if (DELAY > 0) __builtin_amdgcn_s_sleep(DELAY);
#pragma unroll 1
  for (int spins = 0; spins < SPIN_LIMIT; ++spins) {
    unsigned v[N];
#pragma unroll
    for (int k = 0; k < N; ++k) v[k] = __builtin_amdgcn_raw_buffer_load_b32(rf, off + k * 64 * FS, 0, SC1);
    bool ok = true;
#pragma unroll
    for (int k = 0; k < N; ++k) { asm volatile("" : "+v"(v[k]) :: "memory"); ok = ok && v[k] == tag; }
    const u64 miss = __ballot(!ok);
    if (miss == 0) break;
    if (__popcll(miss) > 8) __builtin_amdgcn_s_sleep(8); else __builtin_amdgcn_s_sleep(2);
  }
  asm volatile("" ::: "memory");
}
DI int k1lo(int kap, int lam) { return kap + 32 * lam - ((kap == 0 && lam >= 8) ? 240 : 0); }

constexpr int L_TB = 0;
constexpr int L_SA = 20480;
constexpr int L_A2 = 24576;
constexpr int L_W = 27136;
constexpr int L_E = 47872;
constexpr int L_AP = L_W, L_END = L_E + 20480;
constexpr int L_E2 = L_W, L_MB = L_END;
static_assert(L_MB + 8192 <= LDS_BYTES, "lds");
typedef __attribute__((address_space(3))) void* lds_ptr;
DI unsigned swp(unsigned v) { return __builtin_amdgcn_alignbit(v, v, 16); }
DI unsigned imf(unsigned v) { return swp(v) ^ 0x8000u; }
#define LD4(o) (*(const u32x4*)(lds + (o)))
#define LD1(o) (*(const unsigned*)(lds + (o)))
#define LDC(o) ((u32x4){LD1(o), LD1((o) + 80), LD1((o) + 160), LD1((o) + 240)})

DI void role_b(const int bid, const unsigned epoch, const void* x, const void* wr, const void* wi, void* out, void* ws, char* lds) {
  const int tid = threadIdx.x, l = tid & 63, c16 = l & 15, q = l >> 4;
  const int w = __builtin_amdgcn_readfirstlane(tid >> 6), wh = w >> 1, wl = w & 1;
  const int G = (bid & 1) * 16 + (bid >> 4), eta = (bid >> 3) & 1, sg = (bid >> 1) & 3;
  const rsrc_t rx = mk(x, 2048 * 1024 * 2), rwr = mk(wr, 2048 * 513 * 2), rwi = mk(wi, 2048 * 513 * 2);
  const rsrc_t ro = mk(out, 2048 * 2048 * 2), rz = mk((char*)ws + WS_Z, 4 << 20), rf = mk((char*)ws + WS_FL, FL_BYTES);
  __builtin_amdgcn_s_setprio(3);
  unsigned L[2][8];
  {
    const int off = ((512 * q + 8 * w + sg) * 1024 + 32 * G + 2 * c16) * 2;
#pragma unroll
    for (int j = 0; j < 8; ++j)
#pragma unroll
      for (int n = 0; n < 2; ++n) L[n][j] = __builtin_amdgcn_raw_buffer_load_b32(rx, off + n * 8192, j * 131072, 0);
  }
  SB();
  const float fc = (float)c16;
  const bool u0 = (bid == 0), up = q >= 2;
  const int rd = c16 * 80 + 16 * q, cl = q * 320 + c16 * 4;
  {
    const int n1 = tid >> 4;
    const float rv = (float)n1 * fc * (1.f / 32.f);
    const float c = cosr(rv), sn = c16 == 0 ? __uint_as_float(0x3F800000u | (n1 << 31)) : sinr(-rv);
    const unsigned e = pk(c, sn);
    unsigned short* ap = (unsigned short*)(lds + L_AP + c16 * 80 + n1 * 2);
    ap[0] = (unsigned short)e; ap[640] = (unsigned short)(e >> 16);
    *(unsigned*)(lds + L_A2 + n1 * 80 + c16 * 4) = pk(c * (1.f / 1048576.f), sn * (1.f / 1048576.f));
  }
  u32x4 A1, Mre, Mim;
  {
    const float fa = (float)(16 * q + sg), fk = (float)k1lo(8 * eta + w, c16) * (1.f / 2048.f);
#pragma unroll
    for (int i = 0; i < 4; ++i) {
      const float rv = (fa + (float)(4 * i)) * fk;
      Mre[i] = pk(cosr(rv), sinr(rv));
      Mim[i] = imf(Mre[i]);
    }
    if (w == 0 && eta == 0) {
      const unsigned sel = c16 < 8 ? 0x0c0c0100u : 0x01000c0cu;
#pragma unroll
      for (int i = 0; i < 4; ++i) {
        if (c16 == 0) Mim[i] = (i & 1) ? 0xBF80u : 0x3F80u;
        Mre[i] = __builtin_amdgcn_perm(0u, Mre[i], sel); Mim[i] = __builtin_amdgcn_perm(0u, Mim[i], sel);
      }
    }
  }
  int zst = (128 * eta + 16 * w + 4 * (q & 2)) * 16384 + sg * 4096 + G * 128 + (l & 31) * 4;
  int so[8];
#pragma unroll
  for (int k = 0; k < 8; ++k) { so[k] = k * 16384; PINS(so[k]); }
  int p1w = q * 4608 + wh * 576 + wl * 256 + c16 * 8, p1r = w * 2304 + q * 576 + c16 * 4;
  PIN(Mre); PIN(Mim); PIN(zst); PIN(p1w); PIN(p1r);
  lds_barrier();
  A1 = LD4(L_AP + (c16 & 1) * 1280 + (8 * eta + (c16 >> 1)) * 80 + 16 * q);
  SB();
  u32x4 X[2];
  {
    f32x4 D[2][2];
#pragma unroll
    for (int n = 0; n < 2; ++n) {
#pragma unroll
      for (int p = 0; p < 4; ++p) { X[0][p] = plo(L[n][2 * p], L[n][2 * p + 1]); X[1][p] = phi(L[n][2 * p], L[n][2 * p + 1]); }
      D[n][0] = mma0(A1, X[0]); D[n][1] = mma0(A1, X[1]);
    }
#pragma unroll
    for (int n = 0; n < 2; ++n)
#pragma unroll
      for (int rr = 0; rr < 2; ++rr)
        *(u32x2*)(lds + L_E + p1w + rr * 2304 + n * 128) = (u32x2){pk(D[n][0][2 * rr], D[n][0][2 * rr + 1]), pk(D[n][1][2 * rr], D[n][1][2 * rr + 1])};
  }
  lds_barrier();
  {
    const unsigned* p = (const unsigned*)(lds + L_E + p1r);
    X[0] = (u32x4){p[0], p[32], p[64], p[96]}; X[1] = (u32x4){p[16], p[48], p[80], p[112]};
    SB();
    f32x4 zr[2], zi[2];
#pragma unroll
    for (int h = 0; h < 2; ++h) { zr[h] = mma0(Mre, X[h]); zi[h] = mma0(Mim, X[h]); }
    SB();
#pragma unroll
    for (int r = 0; r < 4; ++r) {
      const auto v = __builtin_amdgcn_permlane16_swap(pk(zr[0][r], zi[0][r]) & ~1u, pk(zr[1][r], zi[1][r]) & ~1u, false, false);
      __builtin_amdgcn_raw_buffer_store_b32(v[0], rz, zst, so[r], SC1);
      __builtin_amdgcn_raw_buffer_store_b32(v[1], rz, zst, so[4 + r], SC1);
    }
  }
  SB();
  const int kb = k1lo(bid >> 4, bid & 15);
  int rbw;
  {
    const int k1 = u0 ? ((0x3120 >> (4 * wh)) & 3) * 256 : kb + 512 * wh;
    rbw = ((wl ? (2048 - k1) & 2047 : k1) * 1026) & ~63;
  }
  PINS(rbw);
  const int l16 = l * 16;
  auto dma_rows = [&](int sa, int sb, int rb) {
    __builtin_amdgcn_raw_ptr_buffer_load_lds(rwr, (lds_ptr)(lds + L_W + sa * 1152), 16, l16, rb, 0, 0);
    __builtin_amdgcn_raw_ptr_buffer_load_lds(rwi, (lds_ptr)(lds + L_W + sb * 1152), 16, l16, rb, 0, 0);
    if (l < 4) {
      __builtin_amdgcn_raw_ptr_buffer_load_lds(rwr, (lds_ptr)(lds + L_W + sa * 1152), 16, l16, rb, 1024, 0);
      __builtin_amdgcn_raw_ptr_buffer_load_lds(rwi, (lds_ptr)(lds + L_W + sb * 1152), 16, l16, rb, 1024, 0);
    }
  };
  const unsigned tag = __builtin_amdgcn_readfirstlane(epoch) + TAG_INC;
  SB();
  drain();
  const int fidx = (G & 7) * 16 + (G >> 3) * 4 + sg;
  if (l == 0) __builtin_amdgcn_raw_buffer_store_b32(tag, rf, FL_B1 + ((8 * eta + w) * 128 + fidx) * FS, 0, SC1);
  SB();
  dma_rows(w, 8 + w, rbw);
  if (__builtin_expect(u0 && w == 1, 0)) dma_rows(16, 17, 1024 * 1026);
  SB();
  {
    const float f1 = ((float)wh + 4.f * fc) * (1.f / 1024.f), fx = (float)(8 * wl + 2 * q);
    char* tp = lds + L_TB + (8 * w + 2 * q) * 80 + c16 * 4;
#pragma unroll
    for (int h = 0; h < 2; ++h) {
      const float p = (fx + (float)h) * f1;
      const unsigned e = pk(cosr(p * 16.f), sinr(p * 16.f));
      *(unsigned*)(tp + h * 80) = e; *(unsigned*)(tp + h * 80 + 5120) = imf(e);
      *(float*)(tp + h * 80 + 10240) = cosr(p); *(float*)(tp + h * 80 + 15360) = sinr(p);
    }
    const int i = tid & 3, b4 = (tid >> 2) & 3, a4 = (tid >> 4) & 3, q2 = w & 3, form = w >> 2;
    const int e = q2 * b4 + i * a4 + form;
    unsigned v = (0x3F80u | ((e & 2) << 14)) << ((e & 1) << 4);
    if (__builtin_expect(u0, 0)) {
      auto coef = [](int s_, int a1, int k1, int j, float& c, float& s) {
        const int e8 = 2 * a1 * k1 + s_ * ((0x3120 >> (4 * j)) & 3) + (j == 0 ? ((s_ & 1) ? 1 : 7) : 0);
        const float rho = j == 0 ? 1.41421356f : 1.f, rv = (float)e8 * 0.125f;
        c = rho * cosr(rv); s = rho * sinr(-rv);
      };
      float c, s;
      coef(q2, i, a4, b4, c, s);
      v = pk(form ? s : c, 0.f) << (b4 >= 2 ? 16 : 0);
      coef(a4, b4, q2, i, c, s);
      const float s2 = i > 0 ? 2.f : 1.f;
      *(unsigned*)(lds + L_SA + 2048 + tid * 4) = ((i >= 2) != (form != 0)) ? pk(s2 * s, s2 * c) : 0u;
    }
    *(unsigned*)(lds + L_SA + tid * 4) = v;
  }
  lds_barrier();
  u32x4 SAre, SAim, SIa, SIb, Bre, Bim, BTre, BTim, Fre, Fim, t1c, t1s, t2c, t2s;
  {
    const int rdk = rd + wh * 1280, clk = cl + wh * 1280;
    SAre = LD4(L_SA + l16); SAim = LD4(L_SA + 1024 + l16);
    Bre = LDC(L_TB + clk); Bim = LDC(L_TB + 5120 + clk); t1c = LDC(L_TB + 10240 + clk); t1s = LDC(L_TB + 15360 + clk);
    Fre = LD4(L_TB + rd); Fim = LD4(L_TB + 5120 + rd);
    BTre = LD4(L_TB + rdk); BTim = LD4(L_TB + 5120 + rdk); t2c = LD4(L_TB + 10240 + rdk); t2s = LD4(L_TB + 15360 + rdk);
    const int si = l16 + (u0 ? 2048 : 0);
    SIa = LD4(L_SA + si); SIb = LD4(L_SA + 1024 + si);
  }
  int z2 = bid * 16384 + q * 4096 + (32 * w + c16) * 4;
  int ewo = q * 4096 + wh * 256 + c16 * 16 + wl * 8, ero = 2048 * w + l16;
  const int k2 = wh + 4 * c16 + 256 * q, cb = (up ? 1024 - k2 : k2) * 2, cs = up ? -128 : 128;
  const int s0 = 4608 * wl + ((kb * 2) & 63), s1 = 4608 * wl + 1152 + ((kb * -2) & 63);
  int wo[4];
#pragma unroll
  for (int r = 0; r < 4; ++r) { wo[r] = (up ? s1 : s0) + cb + r * cs; PIN(wo[r]); }
  unsigned sgn = up ? 0x80000000u : 0u;
  PIN(SAre); PIN(SAim); PIN(Bre); PIN(Bim); PIN(Fre); PIN(Fim); PIN(t1c); PIN(t1s);
  PIN(SIa); PIN(SIb); PIN(BTre); PIN(BTim); PIN(t2c); PIN(t2s); PIN(z2); PIN(ewo); PIN(ero); PIN(sgn);
  const f32x4 T1c = __builtin_bit_cast(f32x4, t1c), T1s = __builtin_bit_cast(f32x4, t1s), T2c = __builtin_bit_cast(f32x4, t2c), T2s = __builtin_bit_cast(f32x4, t2s);
  SB();
  wait_flags<1, 0>(rf, FL_B1 + (l < 32 ? (bid >> 4) * 128 + w * 16 + c16 : (2 * w + (l & 1)) * 128 + fidx) * FS, tag);
  drain();
#pragma unroll
  for (int i = 0; i < 2; ++i)
#pragma unroll
    for (int a = 0; a < 4; ++a) X[i][a] = __builtin_amdgcn_raw_buffer_load_b32(rz, z2 + a * 1024 + i * 64, 0, SC1);
  SB();
  lds_barrier();
  f32x4 Wc[2], Ws[2], Vc, Vs;
  {
    auto ldc = [&](int o) { return __uint_as_float((unsigned)*(const unsigned short*)(lds + L_W + o) << 16); };
    auto lds_ = [&](int o) { return __uint_as_float(((unsigned)*(const unsigned short*)(lds + L_W + o) << 16) + sgn); };
#pragma unroll
    for (int jj = 0; jj < 2; ++jj)
#pragma unroll
      for (int r = 0; r < 4; ++r) { Wc[jj][r] = ldc(wo[r] + 2304 * jj); Ws[jj][r] = lds_(wo[r] + 2304 * jj + 9216); }
    if (wh == 0) {
      const int wx = (up ? s0 : s1) + cb;
#pragma unroll
      for (int jj = 0; jj < 2; ++jj) {
        const float c2 = 0.5f * (Wc[jj][0] + ldc(wx + 2304 * jj)), s2 = 0.5f * (Ws[jj][0] - lds_(wx + 2304 * jj + 9216));
        if ((l & 31) == 0) { Wc[jj][0] = c2; Ws[jj][0] = s2; }
      }
    }
    if (__builtin_expect(u0 && wl == 0, 0)) {
      f32x4 c1, s1_;
#pragma unroll
      for (int r = 0; r < 4; ++r) { c1[r] = ldc(16 * 1152 + cb + r * cs); s1_[r] = lds_(17 * 1152 + cb + r * cs); }
      if (wh == 0 && (l & 31) == 0) s1_[0] = 0.f;
      Vc = 0.5f * (Wc[0] - c1); Vs = 0.5f * (Ws[0] - s1_);
      Wc[0] = 0.5f * (Wc[0] + c1); Ws[0] = 0.5f * (Ws[0] + s1_);
    }
  }
  PIN4(Wc[0]); PIN4(Ws[0]); PIN4(Wc[1]); PIN4(Ws[1]);
  SB();
  {
    f32x4 Dre[2], Dim[2];
#pragma unroll
    for (int i = 0; i < 2; ++i) { Dre[i] = mma0(SAre, X[i]); Dim[i] = mma0(SAim, X[i]); }
#pragma unroll
    for (int r = 0; r < 4; ++r) *(u32x2*)(lds + L_E + ewo + r * 1024) = (u32x2){pk(Dre[0][r], Dim[0][r]), pk(Dre[1][r], Dim[1][r])};
  }
  lds_barrier();
  {
    f32x4 re[2], im[2], Yre[2], Yim[2];
    X[0] = LD4(L_E + ero); X[1] = LD4(L_E + ero + 1024);
    SB();
#pragma unroll
    for (int j = 0; j < 2; ++j) { re[j] = mma0(X[j], Bre); im[j] = mma0(X[j], Bim); }
    SB();
#pragma unroll
    for (int j = 0; j < 2; ++j) {
      const u32x4 y = pk4(dot2(re[j], T1c, im[j], T1s), dot2n(im[j], T1c, re[j], T1s));
      re[j] = mma0(Fre, y); im[j] = mma0(Fim, y);
      SB();
    }
    if (__builtin_expect(u0, 0)) {
      f32x2* mb = (f32x2*)(lds + L_MB);
      if (wl == 0) {
#pragma unroll
        for (int r = 0; r < 4; ++r) mb[k2 + 64 * r] = (f32x2){re[0][r], im[0][r]};
      }
      lds_barrier();
    }
#pragma unroll
    for (int j = 0; j < 2; ++j) {
      Yre[j] = dot2n(re[j], Wc[j], im[j], Ws[j]); Yim[j] = dot2(re[j], Ws[j], im[j], Wc[j]);
      if (j == 0 && __builtin_expect(u0 && wl == 0, 0)) {
        const f32x2* mb = (const f32x2*)(lds + L_MB);
#pragma unroll
        for (int r = 0; r < 4; ++r) {
          const f32x2 m = mb[(1024 - k2 - 64 * r) & 1023];
          Yre[0][r] += m[0] * Vc[r] + m[1] * Vs[r];
          Yim[0][r] += m[0] * Vs[r] - m[1] * Vc[r];
        }
      }
      const u32x4 y = pk4(Yim[j], Yre[j]);
      im[j] = mma0(y, Fre); re[j] = mma0(y, Fim);
      SB();
    }
#pragma unroll
    for (int j = 0; j < 2; ++j) {
      const u32x4 v = pk4(dot2(im[j], T2c, re[j], T2s), dot2n(re[j], T2c, im[j], T2s));
      im[j] = mma0(BTre, v); re[j] = mma0(BTim, v);
      SB();
    }
#pragma unroll
    for (int r = 0; r < 4; ++r) *(u32x2*)(lds + L_E2 + ewo + r * 1024) = (u32x2){pk(im[0][r], re[0][r]), pk(im[1][r], re[1][r])};
  }
  lds_barrier();
  {
    X[0] = LD4(L_E2 + ero); X[1] = LD4(L_E2 + ero + 1024);
    SB();
    f32x4 re[2], im[2];
#pragma unroll
    for (int i = 0; i < 2; ++i) { im[i] = mma0(SIa, X[i]); re[i] = mma0(SIb, X[i]); }
    SB();
    const int z2s = z2 - (q & 1) * (4096 - 64);
#pragma unroll
    for (int a = 0; a < 4; ++a) {
      const auto v = __builtin_amdgcn_permlane16_swap(pk(im[0][a], re[0][a]) | 1u, pk(im[1][a], re[1][a]) | 1u, false, false);
      __builtin_amdgcn_raw_buffer_store_b32(v[0], rz, z2s + a * 1024, 0, SC1);
      __builtin_amdgcn_raw_buffer_store_b32(v[1], rz, z2s + a * 1024, 4096, SC1);
    }
  }
  SB();
  u32x4 Gm = LD4(L_A2 + 1280 * wl + rd), M3[2];
  {
    const float fa = (float)(32 * eta + 4 * (c16 >> 1) + sg) * (1.f / 2048.f);
#pragma unroll
    for (int tt = 0; tt < 2; ++tt) {
      const bool sp = tt == 0 && w == 0;
      const float k0 = (float)k1lo(2 * w + tt, 4 * q), ph = (sp || (c16 & 1) == 0) ? 0.25f : 0.f;
#pragma unroll
      for (int i = 0; i < 4; ++i) {
        const float rv = __builtin_fmaf(fa, k0 + (float)(32 * i), ph);
        M3[tt][i] = pk(cosr(rv), sinr(rv));
      }
    }
    if (w == 0) {
      if (l < 16) M3[0][0] = (c16 & 2) ? 0x3F00BF00u : 0x3F003F00u;
      if (((c16 & 1) != 0) != up) M3[0] = (u32x4){0u, 0u, 0u, 0u};
    }
  }
  int zld = (32 * w + 4 * q) * 16384 + sg * 4096 + G * 128 + c16 * 4;
  int ooff = ((1024 * wl + 256 * q + 32 * eta + 8 * wh + 4 * (c16 >> 3) + sg) * 2048 + 1024 + 32 * G + 4 * (c16 & 7)) * 2;
  int p3w = q * 4384 + wh * 544 + c16 * 16 + wl * 8, p3r = (2 * wh + (c16 >> 3)) * 2192 + q * 544 + (c16 & 7) * 64;
  PIN(M3[0]); PIN(M3[1]); PIN(Gm); PIN(ooff); PIN(p3w); PIN(p3r); PIN(zld);
  SB();
  drain();
  u32x4 Y[4];
  const int zld2 = zld + 16 * 16384;
#pragma unroll 1
  for (int spins = 0; spins < SPIN_LIMIT; ++spins) {
#pragma unroll
    for (int i = 0; i < 4; ++i)
#pragma unroll
      for (int h = 0; h < 2; ++h) {
        Y[h][i] = __builtin_amdgcn_raw_buffer_load_b32(rz, zld + h * 64, so[i], SC1);
        Y[2 + h][i] = __builtin_amdgcn_raw_buffer_load_b32(rz, zld2 + h * 64, so[i], SC1);
      }
    asm volatile("" : "+v"(Y[0]), "+v"(Y[1]), "+v"(Y[2]), "+v"(Y[3]) :: "memory");
    const u32x4 t = Y[0] & Y[1] & Y[2] & Y[3];
    if ((t[0] & t[1] & t[2] & t[3] & 1u) != 0u) break;
    __builtin_amdgcn_s_sleep(2);
  }
  SB();
  {
    f32x4 D[4];
#pragma unroll
    for (int k = 0; k < 4; ++k) D[k] = mma0(M3[k >> 1], Y[k]);
#pragma unroll
    for (int h = 0; h < 2; ++h)
#pragma unroll
      for (int rr = 0; rr < 2; ++rr)
        *(u32x2*)(lds + L_E + p3w + rr * 2192 + h * 256) = (u32x2){pk(D[h][2 * rr], D[h][2 * rr + 1]), pk(D[2 + h][2 * rr], D[2 + h][2 * rr + 1])};
  }
  lds_barrier();
  {
    u32x4 v[4];
#pragma unroll
    for (int c = 0; c < 4; ++c) v[c] = LD4(L_E + p3r + c * 16);
    SB();
    f32x4 O[4];
#pragma unroll
    for (int c = 0; c < 4; ++c) O[c] = mma0(Gm, v[c]);
#pragma unroll
    for (int r = 0; r < 4; ++r)
      __builtin_amdgcn_raw_buffer_store_b64((u32x2){pk(O[0][r], O[1][r]), pk(O[2][r], O[3][r])}, ro, ooff, r * 64 * 4096, NT);
  }
  if (tid == 0 && bid == 0) __builtin_amdgcn_raw_buffer_store_b32(tag, rf, FL_EB, 0, SC1);
}

struct C2 { f32x2 re, im; };
DI C2 cmul(C2 a, C2 b) { C2 o; o.re = a.re * b.re - a.im * b.im; o.im = a.re * b.im + a.im * b.re; return o; }
DI C2 cpow(f32x2 zr, f32x2 zi, float c) {
  C2 o;
#pragma unroll
  for (int e = 0; e < 2; ++e) {
    const float m = __builtin_amdgcn_exp2f(zr[e] * c), a = __builtin_amdgcn_fractf(zi[e] * c);
    o.re[e] = m * cosr(a); o.im[e] = m * sinr(a);
  }
  return o;
}
DI float rbf(float v) { return bflo(pk(v, 0.f)); }
DI float exp_(float v) { return __builtin_amdgcn_exp2f(v * 1.44269504088896f); }

constexpr int O_S2 = 4608, O_S3 = O_S2 + 512, O_R0 = O_S3 + 384, O_R1 = O_R0 + 1216, O_KB = 8576;
constexpr int O_PRG = O_KB + 21 * 528, O_P = O_KB - 2048, O_OLD = O_KB + 6144, O_YB = O_R0, O_NS = O_YB + 1024;
static_assert(O_R1 + 1152 <= O_KB && O_PRG + 4 <= A_SLICE && O_OLD + 4224 <= O_PRG, "slice");

template <int LO, int HI> DI u32x4 band(int sh) {
  constexpr unsigned MASK = ((1u << (HI - LO + 1)) - 1u) << (31 - HI);
  const unsigned bits = (unsigned)(((u64)MASK) >> sh) & 0xffu, s = bits | (bits << 15);
  u32x4 o;
#pragma unroll
  for (int p = 0; p < 4; ++p) o[p] = __umul24(s & (0x10001u << (2 * p)), 0x3F80u >> (2 * p));
  return o;
}

DI u32x2 encode(f32x4 o, u32x2 old) {
  const unsigned y0 = min(pk(o[0], o[1]), 0xfffffffeu), y1 = min(pk(o[2], o[3]), 0xfffffffeu);
  return (u32x2){~(y0 ^ old[0]), ~(y1 ^ old[1])};
}
DI int lane_id() { int l = __builtin_amdgcn_mbcnt_hi(~0u, __builtin_amdgcn_mbcnt_lo(~0u, 0u)); PIN(l); return l; }

template <bool MAIN> DI void role_a(const int a, const int w, const u32x4 par, const unsigned first, const void* dsk,
                                    void* out, void* ws, unsigned* lds_all) {
  const int mu = 2 * (a & 7) + (a >> 7), gam = (a >> 3) & 15;
  const int ch = 64 * gam + 4 * mu + w;
  char* lds = (char*)lds_all + w * A_SLICE;
  const rsrc_t rt = mk((char*)ws + WS_UT, 4 << 20);
  __builtin_amdgcn_s_setprio(3);
  const int l = lane_id(), c16 = l & 15, q = l >> 4;
  const char* gp = lds + 512 - 32 + 32 * c16 + 16 * q;
#define PUT(row, v) (*(u32x2*)(wrp + (row) * 528) = (u32x2){pk((v).re[0], (v).re[1]), pk((v).im[0], (v).im[1])})
  if constexpr (MAIN) {
    const unsigned v_dt = first >> (16 * (ch & 1)), v_lar = par[0], v_aim = par[1], v_cr = par[2], v_ci = par[3];
    const char* rdq = lds + q * 16;
    SB();
    __builtin_amdgcn_s_setprio(2);
    {
      const u32x4 z4 = {0u, 0u, 0u, 0u};
#pragma unroll
      for (int i = 0; i < 3; ++i) *(u32x4*)(lds + O_S2 + l * 16 + i * 1024) = z4;
      if (l < 12) *(u32x4*)(lds + O_S2 + l * 16 + 3072) = z4;
    }
    const int s_ = 527 - c16 + 8 * q;
    int fo = ((s_ & 1) ? O_R1 + (s_ + 1) * 2 : O_R0 + s_ * 2) - 1024 + w * A_SLICE, fo2 = fo + 512;
    int ro0 = (int)((0x889AAABC01234567ull >> (4 * c16)) & 15) * 528, ro2 = (13 + (c16 & 7)) * 528;
    int x0 = (536 - 8 * c16 + 4 * (q & 1) - 256 * (q >> 1)) * 2 - 256;
    int t0o = q == 0 ? O_S2 + 92 + (c16 & 1) * 256 - (c16 >> 1) * 4 : O_S3 + 28 - (c16 >> 1) * 4 + (c16 & 1) * 2;
    PIN(fo); PIN(fo2); PIN(ro0); PIN(ro2); PIN(x0); PIN(t0o);
    SB();
    const float LOG2E = 1.44269504088896f, INV2PI = 0.159154943091895f;
    const float dt = rbf(exp_(bflo(v_dt)));
    const f32x2 zr = {-(rbf(exp_(bflo(v_lar))) * dt) * LOG2E, -(rbf(exp_(bfhi(v_lar))) * dt) * LOG2E};
    const f32x2 zi = {(bflo(v_aim) * dt) * INV2PI, (bfhi(v_aim) * dt) * INV2PI};
    char* wrp = lds + O_KB + l * 8;
    u32x4 H[2][8];
    {
      const C2 E8 = cpow(zr, zi, 8.f), E16 = cmul(E8, E8);
      C2 ha, hb = E8; ha.re = (f32x2){1.f, 1.f}; ha.im = (f32x2){0.f, 0.f};
#pragma unroll
      for (int ct = 0; ct < 2; ++ct) {
#pragma unroll
        for (int b = 0; b < 16; b += 2) {
          if (ct + b > 0) { ha = (ct == 0 && b == 2) ? E16 : cmul(ha, E16); hb = cmul(hb, E16); }
          PUT(b, ha); PUT(b + 1, hb);
        }
        wave_sync();
#pragma unroll
        for (int ks = 0; ks < 8; ++ks) H[ct][ks] = *(const u32x4*)(rdq + O_KB + c16 * 528 + ks * 64);
        wave_sync();
      }
    }
    {
      const f32x2 nzi = -zi;
      const C2 E1 = cpow(zr, nzi, 1.f), E256 = cpow(zr, nzi, 256.f), E2 = cmul(E1, E1);
      C2 g0; g0.re = (f32x2){bflo(v_cr), bfhi(v_cr)} * (2.f * dt); g0.im = (f32x2){bflo(v_ci), bfhi(v_ci)} * (-2.f * dt);
      C2 g1 = cmul(g0, E1), g2 = cmul(g0, E2);
      PUT(0, g0); PUT(1, g1);
      C2 g3 = cmul(g1, E2), g4 = cmul(g2, E2);
      PUT(2, g2); PUT(3, g3);
      C2 g5 = cmul(g3, E2), g6 = cmul(g4, E2);
      PUT(4, g4); PUT(5, g5);
      C2 g7 = cmul(g5, E2), g8 = cmul(g6, E2);
      PUT(6, g6); PUT(7, g7);
      g0 = cmul(g0, E256); g2 = cmul(g2, E256); PUT(8, g0); PUT(9, g2);
      g4 = cmul(g4, E256); g6 = cmul(g6, E256); PUT(10, g4); PUT(11, g6);
      g8 = cmul(g8, E256); g4 = cmul(g4, E256); PUT(12, g8); PUT(13, g4);
      g8 = cmul(g8, E256); g4 = cmul(g4, E256); PUT(14, g8); PUT(15, g4);
      const C2 E512 = cmul(E256, E256);
      g7 = cmul(g8, E256); g8 = cmul(g8, E512); PUT(16, g7); PUT(17, g8);
      g7 = cmul(g7, E512); g8 = cmul(g8, E512); PUT(18, g7); PUT(19, g8);
      g7 = cmul(g7, E512); PUT(20, g7);
    }
    wave_sync();
    SB();
    const unsigned s_ds = sld((const unsigned*)dsk + (ch >> 1));
    u32x4 F[17];
    {
      const char* rp[2] = {rdq + O_KB + ro0, rdq + O_KB + ro2};
      u32x4 gf[6];
#pragma unroll
      for (int k = 0; k < 5; ++k) gf[k] = *(const u32x4*)(rp[0] + k * 64);
      f32x4 Ka[2], Kb[2];
      SB();
#pragma unroll
      for (int k = 0; k < 8; ++k) {
        gf[(k + 5) % 6] = *(const u32x4*)(rp[(k + 5) >> 3] + ((k + 5) & 7) * 64);
        if (k == 0) { Ka[0] = mma0(gf[0], H[0][0]); Ka[1] = mma0(gf[0], H[1][0]); }
        else { Ka[0] = mma(gf[k % 6], H[0][k], Ka[0]); Ka[1] = mma(gf[k % 6], H[1][k], Ka[1]); }
        SB();
      }
      gf[13 % 6] = *(const u32x4*)(rp[1] + 5 * 64);
      Kb[0] = mma0(gf[8 % 6], H[0][0]); Kb[1] = mma0(gf[8 % 6], H[1][0]);
      SB();
#pragma unroll
      for (int ct = 0; ct < 2; ++ct) {
        const unsigned pa = pk(Ka[ct][0], Ka[ct][1]), pb = pk(Ka[ct][2], Ka[ct][3]);
        char* p = lds + x0 + 256 * (1 - ct);
        *(u32x2*)(p + O_R0) = (u32x2){pa, pb};
        *(unsigned short*)(p + O_R1 + 2) = (unsigned short)pa;
        *(unsigned*)(p + O_R1 + 4) = __builtin_amdgcn_alignbit(pb, pa, 16);
        *(unsigned short*)(p + O_R1 + 8) = (unsigned short)(pb >> 16);
        if (ct == 1 && l == 47) {
          const unsigned v = plo(pa, pa);
          *(unsigned short*)(lds + O_R0 + 58) = (unsigned short)v; *(unsigned*)(lds + O_R0 + 60) = v;
          *(unsigned*)(lds + O_R1 + 60) = v; *(unsigned short*)(lds + O_R1 + 64) = (unsigned short)v;
        }
      }
      wave_sync();
      SB();
      const char* fp = (const char*)lds_all + fo;
      const char* fp2 = (const char*)lds_all + fo2;
      auto ldf = [&](int i) {
#pragma unroll
        for (int p = 0; p < 4; ++p) F[i][p] = i > 8 ? *(const unsigned*)(fp + 1024 - 64 * i + 4 * p) : *(const unsigned*)(fp2 + 512 - 64 * i + 4 * p);
      };
#pragma unroll
      for (int k = 1; k < 8; ++k) {
        if (k < 3) gf[(k + 13) % 6] = *(const u32x4*)(rp[1] + (k + 5) * 64);
        Kb[0] = mma(gf[(k + 8) % 6], H[0][k], Kb[0]); Kb[1] = mma(gf[(k + 8) % 6], H[1][k], Kb[1]);
        ldf(2 * k - 2); ldf(2 * k - 1);
        SB();
      }
      ldf(14); ldf(15); ldf(16);
      if (q < 2) {
#pragma unroll
        for (int ct = 0; ct < 2; ++ct) {
          const unsigned za = pk(Kb[ct][0], Kb[ct][1]), zb = pk(Kb[ct][2], Kb[ct][3]);
          char* p = lds + t0o + 32 * (1 - ct);
          if (q == 0) { *(unsigned*)(p + 64) = za; *(unsigned*)p = zb; }
          else {
            *(unsigned short*)(p + 192) = (unsigned short)za; *(unsigned short*)(p + 128) = (unsigned short)(za >> 16);
            *(unsigned short*)(p + 64) = (unsigned short)zb; *(unsigned short*)p = (unsigned short)(zb >> 16);
          }
        }
      }
    }
    const unsigned v_ds = (s_ds >> (16 * (ch & 1))) & 0xffffu;
    unsigned dv = (q == 2 + (c16 >> 3)) ? v_ds << (16 * (c16 & 1)) : 0u;
    u32x4 Dd;
#pragma unroll
    for (int p = 0; p < 4; ++p) Dd[p] = ((c16 >> 1) & 3) == p ? dv : 0u;
    PIN(Dd);
#pragma unroll
    for (int i = 0; i < 17; ++i) PIN(F[i]);
    int pos = w * A_SLICE + 32 * c16 + 8 * q, lin = w * A_SLICE + l * 16;
    PIN(pos); PIN(lin);
    __builtin_amdgcn_s_setprio(1);
    lds_barrier();
    f32x4 acc[8];
#pragma unroll
    for (int T = 0; T < 8; ++T) acc[T] = (f32x4){0.f, 0.f, 0.f, 0.f};
    {
      char* const lb = (char*)lds_all;
      constexpr int PD = 6;
      constexpr int MB = 29;
      u32x4 G[PD + 1];
      u32x2 old, yy;
      f32x4 far;
#pragma unroll
      for (int k = 0; k < PD; ++k) G[k] = *(const u32x4*)(gp + 64 * (k - 7));
      sfor<0, 68>([&](auto mc) {
        constexpr int k = decltype(mc)::value, m = k - 7;
        if constexpr (k + PD < 64) G[(k + PD) % (PD + 1)] = *(const u32x4*)(gp + 64 * (m + PD));
        if constexpr (m == MB) {
          lds_barrier();
          acc[6] = *(const f32x4*)(lb + lin + O_P + 1024 * 6);
        }
        if constexpr (m == 36) acc[7] = *(const f32x4*)(lb + lin + O_P + 1024 * 7);
        if constexpr (k < 64) {
          if constexpr (m >= 0 && (m & 7) == 0) acc[m >> 3] = mma(Dd, G[k % (PD + 1)], acc[m >> 3]);
          sfor<0, 3>([&](auto ic) {
            constexpr int i = ((64 - m) & 7) + 8 * decltype(ic)::value;
            if constexpr (i <= 16 && m + i >= 0 && m + i <= 56) acc[(m + i) >> 3] = mma(F[i], G[k % (PD + 1)], acc[(m + i) >> 3]);
          });
        }
        constexpr int T = (m + 2) >> 3, d = m - 8 * T;
        if constexpr (T >= 0 && T < 8) {
          if constexpr (T == 2 || T == 3) {
            if constexpr (d == 2) *(f32x4*)(lb + lin + O_NS + 1024 * (T - 2)) = acc[T];
          } else {
            if constexpr (d == -2) {
              old = *(const u32x2*)(lb + pos + 512 + 512 * T);
              if constexpr (T == 4 || T == 5) far = *(const f32x4*)(lb + lin + O_P + 1024 * T);
            }
            if constexpr (d == 2 && (T == 4 || T == 5)) { acc[T][0] = opq(acc[T][0] + far[0]); acc[T][1] = opq(acc[T][1] + far[1]); acc[T][2] = opq(acc[T][2] + far[2]); acc[T][3] = opq(acc[T][3] + far[3]); }
            if constexpr (d == 3) { yy[0] = min(pk(acc[T][0], acc[T][1]), 0xfffffffeu); yy[1] = min(pk(acc[T][2], acc[T][3]), 0xfffffffeu); PIN(yy); }
            if constexpr (d == 4) {
              *(u32x2*)(lb + pos + (T < 2 ? O_YB + 512 * T : O_P + 1024 * (T & 6) + 512 * (T & 1))) = ~(yy ^ old);
              wave_sync();
              if constexpr (T > 4 && (T & 1)) *(volatile __attribute__((address_space(3))) int*)(lds + O_PRG) = T;
            }
          }
        }
        MIX(2);
        SB();
      });
    }
  } else {
    const int sh = 15 - c16 + 8 * q;
    const rsrc_t ro = mk(out, 2048 * 2048 * 2), rf = mk((char*)ws + WS_FL, FL_BYTES);
    const int t0 = 512 * w + 32 * mu;
    int toff = gam * 262144 + q * 16384 + (w * 16 + mu) * 256 + c16 * 16;
    int bw = (l & 7) * 528 + (l >> 3) * 8;
    const int br = l * 16 + (l >> 5) * 16;
    if (l == 0) *(int*)(lds + O_PRG) = 3;
    PIN(toff); PIN(bw);
    SB();
    u32x4 old[4];
    {
      char* bb = lds;
      drain();
      u32x4 L[4];
#pragma unroll
      for (int j = 0; j < 4; ++j) L[j] = *(const u32x4*)(bb + j * 1024 + l * 16);
      wave_sync();
#pragma unroll
      for (int p = 0; p < 4; ++p) {
        *(u32x2*)(bb + bw + (2 * p) * 64) = (u32x2){plo(L[0][p], L[1][p]), plo(L[2][p], L[3][p])};
        *(u32x2*)(bb + bw + (2 * p + 1) * 64) = (u32x2){phi(L[0][p], L[1][p]), phi(L[2][p], L[3][p])};
      }
      wave_sync();
#pragma unroll
      for (int i = 0; i < 4; ++i) old[i] = *(const u32x4*)(bb + br + i * 1056);
#pragma unroll
      for (int i = 0; i < 4; ++i) __builtin_amdgcn_raw_buffer_store_b128(old[i], rt, toff, i * 65536, SC1);
      wave_sync();
    }
    SB();
    const unsigned tag = __builtin_amdgcn_readfirstlane(first) + TAG_INC;
    u32x4 M34 = band<3, 4>(sh), B35 = band<3, 5>(sh), B610 = band<6, 10>(sh), Bod = band<5, 11>(sh), Bev = band<4, 12>(sh);
#pragma unroll
    for (int p = 0; p < 4; ++p) M34[p] = (M34[p] >> 7) & 0x10001u;
    PIN(M34); PIN(B35); PIN(B610); PIN(Bod); PIN(Bev);
    SB();
    drain();
    if (l == 0) __builtin_amdgcn_raw_buffer_store_b32(tag, rf, FL_A1 + (gam * 64 + w * 16 + mu) * FS, 0, SC1);
    if (l < 32) *(u32x4*)(lds + l * 16) = (u32x4){0u, 0u, 0u, 0u};
    wait_flags<1, 16>(rf, FL_A1 + (gam * 64 + (l < 32 ? w * 16 + c16 : (l & 3) * 16 + mu)) * FS, tag);
    {
      u32x4 U[4];
#pragma unroll
      for (int i = 0; i < 4; ++i) U[i] = __builtin_amdgcn_raw_buffer_load_b128(rt, ch * 4096 + l * 16 + i * 1024, 0, SC1);
#pragma unroll
      for (int i = 0; i < 4; ++i) *(u32x4*)((char*)lds_all + ((l >> 2) & 3) * A_SLICE + 512 + w * 1024 + i * 256 + q * 64 + (l & 3) * 16) = U[i];
    }
    __builtin_amdgcn_s_setprio(2);
    lds_barrier();
    f32x4 acc[8];
    int ko = w * A_SLICE + O_S2 + (15 - c16 + 4 * q) * 4;
    PIN(ko);
    const unsigned* kp = (const unsigned*)((const char*)lds_all + ko);
    auto ldk = [&](int o) { return (u32x4){kp[o], kp[o + 1], kp[o + 2], kp[o + 3]}; };
    auto ldg = [&](int B, int sh24) { return *(const u32x4*)(gp + 512 * B - sh24); };
    u32x4 KX[3], KY[3], G[2], G24[2], VX, VY;
    f32x4 xa[2], xb[2];
#define CV2(dst, p, q_, h) do { dst[2 * (h)] = pk(p[2 * (h)], q_[2 * (h)]); dst[2 * (h) + 1] = pk(p[2 * (h) + 1], q_[2 * (h) + 1]); } while (0)
    {
      u32x4 g[4], g24[4], KZ[4], VZ[4];
#pragma unroll
      for (int B = 0; B < 4; ++B) { g[B] = ldg(B, 0); g24[B] = ldg(B, 16); }
      const unsigned ka = *(const unsigned*)(lds + O_S2 + 256 + 64);
#pragma unroll
      for (int d = 0; d < 4; ++d) KZ[d] = ldk(176 - 16 * d);
      SB();
      f32x4 va[4], vb[4];
      u32x4 F32;
#pragma unroll
      for (int B = 0; B < 4; ++B) {
        va[B] = mma0(g[B], Bod); *(u32x4*)(lds + O_OLD + l * 16 + B * 1024) = old[B]; SB();
        vb[B] = mma0(g24[B], Bev); F32[B] = M34[B] * (ka >> 16); SB();
      }
      xa[0] = mma0(g[0], B35); CV2(VZ[0], va[0], vb[0], 0); SB();
      xa[1] = mma0(g[0], B610); CV2(VZ[0], va[0], vb[0], 1); SB();
      xb[0] = mma0(g24[0], B35); CV2(VZ[1], va[1], vb[1], 0); SB();
      xb[1] = mma0(g24[0], B610); CV2(VZ[1], va[1], vb[1], 1); SB();
#pragma unroll
      for (int B = 0; B < 4; ++B) { acc[B + 4] = mma0(F32, g[B]); CV2(VZ[2 + (B >> 1)], va[2 + (B >> 1)], vb[2 + (B >> 1)], B & 1); SB(); }
      G[0] = g[2]; G24[0] = g24[2]; G[1] = g[1]; G24[1] = g24[1];
      sfor<0, 10>([&](auto nc) {
        constexpr int n = decltype(nc)::value, B = n < 4 ? 0 : (n < 7 ? 1 : (n < 9 ? 2 : 3)), d = n - (B == 0 ? 0 : (B == 1 ? 4 : (B == 2 ? 7 : 9)));
        acc[B + d + 4] = mma(VZ[B], KZ[d], acc[B + d + 4]);
        if constexpr (n < 2) CV2(VX, xa[0], xa[1], n);
        if constexpr (n >= 2 && n < 5) KX[n - 2] = ldk(32 - 16 * (n - 2));
        if constexpr (n >= 5 && n < 8) KY[n - 5] = ldk(96 - 16 * (n - 5));
        SB();
      });
    }
    {
      acc[2] = acc[3] = (f32x4){0.f, 0.f, 0.f, 0.f};
      sfor<0, 6>([&](auto bc) {
        constexpr int B = decltype(bc)::value, N = B + 1;
        if constexpr (N < 6) { xa[0] = mma0(G[N & 1], B35); SB(); xa[1] = mma0(G[N & 1], B610); if constexpr (N + 2 < 6) G[N & 1] = ldg(N + 2, 0); SB(); }
        sfor<0, 3>([&](auto dc) {
          constexpr int d = decltype(dc)::value;
          if constexpr (B + d + 2 <= 7) acc[B + d + 2] = mma(VX, KX[d], acc[B + d + 2]);
          if constexpr (d < 2) CV2(VY, xb[0], xb[1], d);
          if constexpr (d == 2 && B >= 1) *(f32x4*)(lds + O_P + (B + 1) * 1024 + l * 16) = acc[B + 1];
          SB();
        });
        if constexpr (N < 6) { xb[0] = mma0(G24[N & 1], B35); SB(); xb[1] = mma0(G24[N & 1], B610); if constexpr (N + 2 < 6) G24[N & 1] = ldg(N + 2, 16); SB(); }
        sfor<0, 3>([&](auto dc) {
          constexpr int d = decltype(dc)::value;
          if constexpr (B + d + 2 <= 7) acc[B + d + 2] = mma(VY, KY[d], acc[B + d + 2]);
          if constexpr (d < 2 && N < 6) CV2(VX, xa[0], xa[1], d);
          SB();
        });
      });
    }
#undef CV2
    *(f32x4*)(lds + O_P + 7 * 1024 + l * 16) = acc[7];
    lds_barrier();
    const int l2 = lane_id();
    if (l2 == 0 && w == 0 && mu == 0) __builtin_amdgcn_raw_buffer_store_b32(tag, rf, FL_EA + gam * 256, 0, SC1);
    const int yoff = (ch - w) * 4096 + l2 * 16;
    const char* const gs = (const char*)lds_all + ((l2 >> 2) & 3) * A_SLICE;
    const int gt = (l2 >> 4) * 64 + (l2 & 3) * 16;
    {
      __builtin_amdgcn_raw_buffer_store_b128(*(const u32x4*)(gs + O_YB + w * 256 + gt), rt, yoff + w * 1024, 0, SC1);
      const int t = (w * 256 + gt) >> 1, slot = (t >> 8) * 1024 + ((t >> 2) & 3) * 256 + ((t >> 4) & 15) * 16;
      const u32x4 o = *(const u32x4*)(gs + 1536 + w * 256 + gt);
      const u32x2 y0 = encode(*(const f32x4*)(gs + O_NS + slot) + *(const f32x4*)(gs + O_P + 2048 + slot), (u32x2){o[0], o[1]});
      const u32x2 y1 = encode(*(const f32x4*)(gs + O_NS + slot + 256) + *(const f32x4*)(gs + O_P + 2048 + slot + 256), (u32x2){o[2], o[3]});
      __builtin_amdgcn_raw_buffer_store_b128((u32x4){y0[0], y0[1], y1[0], y1[1]}, rt, yoff + 4096 + w * 1024, 0, SC1);
    }
    __builtin_amdgcn_s_setprio(3);
    if (w >= 2) {
#pragma unroll
      for (int P = 2; P < 4; ++P) {
#pragma unroll 1
        for (int spins = 0; spins < SPIN_LIMIT; ++spins) {
          if (__all(*(volatile __attribute__((address_space(3))) int*)(gs + O_PRG) > 2 * P)) break;
          __builtin_amdgcn_s_sleep(1);
        }
        wave_sync();
#pragma unroll
        for (int e = 0; e < 2; ++e)
          __builtin_amdgcn_raw_buffer_store_b128(*(const u32x4*)(gs + O_P + 2048 * P + (w & 1) * 512 + e * 256 + gt), rt, yoff + 4096 * P + (w & 1) * 2048 + e * 1024, 0, SC1);
      }
    }
    const int toff2 = gam * 262144 + (l2 >> 4) * 16384 + (w * 16 + mu) * 256 + (l2 & 15) * 16;
    const int bw2 = (l2 & 7) * 528 + (l2 >> 3) * 8, br2 = l2 * 16 + (l2 >> 5) * 16;
#pragma unroll
    for (int i = 0; i < 4; ++i) old[i] = *(const u32x4*)(lds + O_OLD + l2 * 16 + i * 1024);
    if (w != 2) __builtin_amdgcn_s_sleep(24);
    u32x4 v[4];
#pragma unroll 1
    for (int spins = 0; spins < SPIN_LIMIT; ++spins) {
#pragma unroll
      for (int i = 0; i < 4; ++i) v[i] = __builtin_amdgcn_raw_buffer_load_b128(rt, toff2, i * 65536, SC1);
      asm volatile("" : "+v"(v[0]), "+v"(v[1]), "+v"(v[2]), "+v"(v[3]) :: "memory");
      unsigned mx = 0;
#pragma unroll
      for (int i = 0; i < 16; ++i) { v[i >> 2][i & 3] = ~(v[i >> 2][i & 3] ^ old[i >> 2][i & 3]); mx = max(mx, v[i >> 2][i & 3]); }
      if (mx != 0xffffffffu) break;
      __builtin_amdgcn_s_sleep(8);
    }
    {
      char* bb = lds + O_OLD;
      wave_sync();
#pragma unroll
      for (int i = 0; i < 4; ++i) *(u32x4*)(bb + br2 + i * 1056) = v[i];
      wave_sync();
      u32x2 X[8];
#pragma unroll
      for (int c = 0; c < 8; ++c) X[c] = *(const u32x2*)(bb + bw2 + c * 64);
      const int off = ((t0 + 4 * (l2 >> 3)) * 2048 + 64 * gam) * 2 + (l2 & 7) * 16;
#pragma unroll
      for (int h = 0; h < 2; ++h) {
        const u32x4 e = {plo(X[0][h], X[1][h]), plo(X[2][h], X[3][h]), plo(X[4][h], X[5][h]), plo(X[6][h], X[7][h])};
        const u32x4 o = {phi(X[0][h], X[1][h]), phi(X[2][h], X[3][h]), phi(X[4][h], X[5][h]), phi(X[6][h], X[7][h])};
        __builtin_amdgcn_raw_buffer_store_b128(e, ro, off, (2 * h) * 4096, NT);
        __builtin_amdgcn_raw_buffer_store_b128(o, ro, off, (2 * h + 1) * 4096, NT);
      }
    }
  }
#undef PUT
}

__global__ void __launch_bounds__(512, 4) fused_kernel(const void* u, const void* lar, const void* aim, const void* cr, const void* ci,
                                                       const void* ldt, const void* dsk, const void* x, const void* wr, const void* wi,
                                                       void* out, void* ws) {
  __shared__ __attribute__((aligned(16))) unsigned lds[LDS_BYTES / 4];
  const int bid = blockIdx.x;
  const int w8 = __builtin_amdgcn_readfirstlane(threadIdx.x >> 6);
  const int a = bid & 255, mu = 2 * (a & 7) + (a >> 7), gam = (a >> 3) & 15, ch = 64 * gam + 4 * mu + (w8 & 3), l = threadIdx.x & 63;
  const bool gen = bid >= 256 && w8 < 4;
  const int poff = gen ? ch * 256 + l * 4 : 0x7ffffff0;
  const int gm = gen ? -1 : 0, fo = (((ch >> 1) * 4) & gm) | (((bid >> 8) * (FL_EA + gam * 256)) & ~gm);
  const unsigned first = __builtin_amdgcn_raw_buffer_load_b32(mk(gen ? ldt : (const char*)ws + WS_FL, gen ? 2048 : FL_BYTES), 0, fo, SC1);
  u32x4 par;
  par[0] = __builtin_amdgcn_raw_buffer_load_b32(mk(lar, 262144), poff, 0, 0);
  par[1] = __builtin_amdgcn_raw_buffer_load_b32(mk(aim, 262144), poff, 0, 0);
  par[2] = __builtin_amdgcn_raw_buffer_load_b32(mk(cr, 262144), poff, 0, 0);
  par[3] = __builtin_amdgcn_raw_buffer_load_b32(mk(ci, 262144), poff, 0, 0);
  if (bid >= 256 && w8 >= 4) {
    const int off = ((512 * (w8 & 3) + 32 * mu + 4 * (l >> 3)) * 1024 + 64 * gam) * 2 + (l & 7) * 16;
#pragma unroll
    for (int j = 0; j < 4; ++j)
      __builtin_amdgcn_raw_ptr_buffer_load_lds(mk(u, 2048 * 1024 * 2), (lds_ptr)((char*)lds + (w8 & 3) * A_SLICE + j * 1024), 16, off, j * 2048, 0, 0);
  }
  int role = bid;
  PINS(role);
  SB();
  if (__builtin_expect(role < 256, 1)) role_b(bid, first, x, wr, wi, out, ws, (char*)lds);
  else if (__builtin_expect(w8 < 4, 1)) role_a<true>(a, w8 & 3, par, first, dsk, out, ws, lds);
  else role_a<false>(a, w8 & 3, par, first, dsk, out, ws, lds);
}

extern "C" void kernel_launch(void* const* d_in, const int* in_sizes, int n_in, void* d_out, int out_size, void* d_ws, size_t ws_size,
                              hipStream_t stream) {
  fused_kernel<<<512, 512, 0, stream>>>(d_in[0], d_in[1], d_in[2], d_in[3], d_in[4], d_in[5], d_in[6], d_in[7], d_in[8], d_in[9], d_out, d_ws);
}
```
